```python
import torch

def _softmax(x, axis=-1):
    return torch.softmax(x, dim=axis)
def _gelu(x):
    return torch.nn.functional.gelu(x, approximate="tanh")
def _silu(x):
    return torch.nn.functional.silu(x)
def _sigmoid(x):
    return torch.sigmoid(x)
def _layernorm(x, axis=-1, eps=1e-5):
    m = x.mean(dim=axis, keepdim=True)
    v = x.var(dim=axis, keepdim=True, unbiased=False)
    return (x - m) / torch.sqrt(v + eps)
def _scatter_add(tgt, idx, src, axis=0):
    return tgt.clone().index_add_(axis, idx, src)
def _segment_sum(src, seg, n):
    out = torch.zeros((n,) + src.shape[1:], dtype=src.dtype)
    return out.index_add_(0, seg, src)
def _pad(x, pw):
    flat = tuple(v for pair in reversed(pw) for v in pair)
    return torch.nn.functional.pad(x, flat)

kernel_name = 'gated_mix__softmax_full_mha__swiglu_ffn__prompt'

def setup_inputs():

    _w = (lambda a: (lambda t: t.to(torch.bfloat16) if t.is_floating_point() else t)(torch.as_tensor(a)))
    def _A_setup():
        B, S, D = 1, 2048, 448
        return {
            "x": _w((lambda *s: torch.randn(*s, dtype=torch.bfloat16) * 0.05)(B, S, D)),
            "wqkv": _w((lambda *s: torch.randn(*s, dtype=torch.bfloat16) * 0.8)(D, 3 * D)),
            "wo": _w((lambda *s: torch.randn(*s, dtype=torch.bfloat16) * 0.05)(D, D)),
        }
    _A_in = _A_setup()

    def _B_setup():
        B, S, D, F = 1, 2048, 448, 1792
        return {
            "x": _w((lambda *s: torch.randn(*s, dtype=torch.bfloat16) * 0.05)(B, S, D)),
            "w_gate": _w((lambda *s: torch.randn(*s, dtype=torch.bfloat16) * 0.1)(D, F)),
            "w_up": _w((lambda *s: torch.randn(*s, dtype=torch.bfloat16) * 0.1)(D, F)),
            "w_down": _w((lambda *s: torch.randn(*s, dtype=torch.bfloat16) * 0.1)(F, D)),
        }
    _B_in = _B_setup()

    return {
        "A_x": _A_in["x"],
        "A_wqkv": _A_in["wqkv"],
        "A_wo": _A_in["wo"],
        "B_x": _B_in["x"],
        "B_w_gate": _B_in["w_gate"],
        "B_w_up": _B_in["w_up"],
        "B_w_down": _B_in["w_down"],
        "gate_w": _w((lambda *s: torch.randn(*s, dtype=torch.bfloat16) * 0.05)(448) * 16.0),
    }

def reference(A_x, A_wqkv, A_wo, B_x, B_w_gate, B_w_up, B_w_down, gate_w):
    def _A_ref(x, wqkv, wo):
        B, S, D = x.shape
        H = 16; Dh = D // H
        qkv = (lambda x, *d: x.permute(*d))((x @ wqkv).reshape(B, S, 3, H, Dh), 2, 0, 3, 1, 4)
        q, k, v = qkv[0], qkv[1], qkv[2]
        scale = Dh ** -0.5
        scores = torch.einsum("bhqd,bhkd->bhqk", q, k) * scale
        p = _softmax(scores, axis=-1)
        o = torch.einsum("bhqk,bhkd->bhqd", p, v)
        return (lambda x, *d: x.permute(*d))(o, 0, 2, 1, 3).reshape(B, S, D) @ wo

    def _B_ref(x, w_gate, w_up, w_down):
        g = _silu(x @ w_gate)
        u = x @ w_up
        return (g * u) @ w_down

    x = A_x
    g = _sigmoid((x * gate_w).sum(-1, keepdims=True))
    a = _A_ref(x, A_wqkv, A_wo)
    b = _B_ref(x, B_w_gate, B_w_up, B_w_down)
    return g * a + (1 - g) * b
```

```cpp
#include <hip/hip_runtime.h>
#include <stdint.h>
#include <type_traits>

typedef unsigned short u16;
typedef unsigned long long u64;
typedef __attribute__((ext_vector_type(8))) short bf16x8;
typedef __attribute__((ext_vector_type(4))) short s16x4;
typedef __attribute__((ext_vector_type(16))) float f32x16;
typedef __attribute__((ext_vector_type(4))) float f32x4;
typedef __attribute__((ext_vector_type(4))) unsigned u32x4;
#define LAS __attribute__((address_space(3)))
#define GAS __attribute__((address_space(1)))
typedef LAS char* lptr;
typedef __amdgpu_buffer_rsrc_t rsrc_t;

constexpr unsigned WS_EPOCH = 0;
constexpr unsigned FS = 256;
constexpr unsigned WS_F1 = 17u << 20;
constexpr unsigned WS_F2 = WS_F1 + 256 * FS;
constexpr unsigned WS_G = 16384;
constexpr unsigned WS_K = 3u << 20;
constexpr unsigned WS_V = WS_K + 2048;
constexpr unsigned HS = 262144, TS = 4096;
constexpr unsigned WS_O = 7u << 20;
constexpr unsigned WS_H = 9u << 20;
constexpr unsigned WS_END = 16u << 20;
constexpr int CODE_LINES = 280;

#define MFMA(a, b, c) __builtin_amdgcn_mfma_f32_32x32x16_bf16(a, b, c, 0, 0, 0)
#define SBAR() __builtin_amdgcn_sched_barrier(0)
#define PIN(x) asm volatile("" : "+v"(x))
#define RLX_AGENT __ATOMIC_RELAXED, __HIP_MEMORY_SCOPE_AGENT
#define EXP2(x) __builtin_amdgcn_exp2f(x)
#define BALLOT(p) __builtin_amdgcn_ballot_w64(p)
#define ALL(p) (BALLOT(p) == ~0ull)
#define ANY(p) (BALLOT(p) != 0ull)
__device__ __forceinline__ unsigned F2U(float f) { return __builtin_bit_cast(unsigned, f); }
__device__ __forceinline__ float U2F(unsigned u) { return __builtin_bit_cast(float, u); }
__device__ __forceinline__ int imin(int a, int b) { return a < b ? a : b; }

__device__ __forceinline__ unsigned cvtpk(float lo, float hi) {
    typedef __attribute__((ext_vector_type(2))) float f2; typedef __attribute__((ext_vector_type(2))) __bf16 b2;
    f2 v = {lo, hi}; b2 r = __builtin_convertvector(v, b2); return __builtin_bit_cast(unsigned, r);
}
__device__ __forceinline__ void dma16(unsigned voff, const void* sbase, unsigned ldsaddr) {
    asm volatile("s_mov_b32 m0, %2\n\ts_nop 0\n\tglobal_load_lds_dwordx4 %0, %1" ::"v"(voff), "s"(sbase), "s"(ldsaddr) : "memory");
}
__device__ __forceinline__ s16x4 trd(lptr p) {
    typedef short v4i16_t __attribute__((ext_vector_type(4)));
    return __builtin_bit_cast(s16x4, __builtin_amdgcn_ds_read_tr16_b64_v4i16((LAS v4i16_t*)p));
}
__device__ __forceinline__ bf16x8 trfrag(lptr p0, lptr p1) {
    s16x4 a = trd(p0), b = trd(p1);
    return (bf16x8){a[0], a[1], a[2], a[3], b[0], b[1], b[2], b[3]};
}
template <int N> __device__ __forceinline__ void wait_bar() {
    asm volatile("s_waitcnt vmcnt(%0) lgkmcnt(0)\n\ts_barrier" ::"i"(N) : "memory");
}
__device__ __forceinline__ float silu_mul(float g, float u) {
    float e = EXP2(g * -1.4426950408889634f);
    return g * __builtin_amdgcn_rcpf(1.0f + e) * u;
}
__device__ __forceinline__ bf16x8 ld_frag(rsrc_t rs, unsigned voff, unsigned soff) {
    return __builtin_bit_cast(bf16x8, __builtin_amdgcn_raw_buffer_load_b128(rs, voff, soff, 16));
}
template <int SLEEP = 1>
__device__ __forceinline__ void poll_flags(const char* ws, unsigned off, u64 tag) {
    GAS u64* p = (GAS u64*)(ws + off);
#pragma unroll 1
    for (unsigned spins = 0; spins < (1u << 18); ++spins) {
        u64 v = __hip_atomic_load(p, RLX_AGENT);
        if (ALL(v == tag)) break;
        __builtin_amdgcn_s_sleep(SLEEP);
    }
    __builtin_amdgcn_fence(__ATOMIC_ACQUIRE, "wavefront");
}
__device__ __forceinline__ void set_flag(char* ws, unsigned off, u64 tag) {
    asm volatile("s_mov_b64 exec, 1\n\tglobal_store_dwordx2 %0, %1, %2 sc1\n\ts_mov_b64 exec, -1" ::"v"(off), "v"(tag), "s"(ws) : "memory");
}
__device__ __forceinline__ float swap_max(float m) {
    auto rr = __builtin_amdgcn_permlane32_swap(F2U(m), F2U(m), false, false);
    return __builtin_fmaxf(U2F(rr[0]), U2F(rr[1]));
}
#define MX3(a, b, c) __builtin_fmaxf(__builtin_fmaxf((a), (b)), (c))
__device__ __forceinline__ float max16(const f32x16& p) {
    float a = MX3(p[0], p[1], p[2]), b = MX3(p[3], p[4], p[5]);
    a = MX3(a, p[6], p[7]); b = MX3(b, p[8], p[9]); a = MX3(a, p[10], p[11]); b = MX3(b, p[12], p[13]); a = MX3(a, p[14], p[15]);
    return __builtin_fmaxf(a, b);
}

__host__ __device__ __forceinline__ int a_role(int xcd, int slot) {
    const int p = xcd >> 1, m = -(int)(slot >= 24);
    const int lo = (3 * p + (slot >> 3)) * 16 + (slot & 7), hi = ((slot >> 2) * 2 + (p >> 1)) * 16 + 4 * (p & 1) + (slot & 3);
    return ((lo & ~m) | (hi & m)) + (xcd & 1) * 8;
}
__attribute__((always_inline)) constexpr int upos(int u) { return (u < 45 ? u % 15 : u - 44) * 8192; }
constexpr int L_QX = 0;
constexpr int L_WQ = 15 * 8192;
constexpr int RING = 8192;
constexpr int L_ATT = 8 * 8192;
__device__ __forceinline__ int bred_off(int idx) { return idx < 10 ? 14 * 8192 + idx * 4096 : (idx - 10) * 4096; }
constexpr int L_CTL = 19 * 8192;
enum { C_KV = 0, C_P2 = 1, C_H = 2, C_QRD = 4, C_ATT = 5, C_BRED = 6, C_ST = 7, C_FIN = 8, C_QX = 12, C_G = 14 };
constexpr int L_HS = 18 * 8192 + 4096;
constexpr int L_GV = L_CTL + 1024;
constexpr int L_M = L_CTL + 256;

struct ASrc { const char* xb; const char* wb[3]; const char* wq; unsigned vx, vw0, vw1, vw2, vq, ldsw; int hf; };

template <int U>
__device__ __forceinline__ void issue_unit(const ASrc& s) {
    constexpr int dst = upos(U);
    if constexpr (U < 35) {
        constexpr int st = U / 5, k = U % 5;
        if constexpr (k < 2) dma16(s.vx, s.xb + (k * 64 * 896 + st * 128), s.ldsw + dst);
        else if constexpr (k == 2) dma16(s.vw0, s.wb[0] + st * (64 * 2688), s.ldsw + dst);
        else dma16(k == 3 ? s.vw1 : s.vw2, s.wb[k - 2] + st * (64 * 3584), s.ldsw + dst);
    } else {
        constexpr int m = U - 35;
        dma16(m % 2 == 0 ? s.vw1 : s.vw2, s.wb[1 + m % 2] + (128 + (m / 2) * (64 * 3584)), s.ldsw + dst);
    }
}
template <int U0, int U1>
__device__ __forceinline__ void issue_units(const ASrc& s) {
    if constexpr (U0 < U1) { issue_unit<U0>(s); issue_units<U0 + 1, U1>(s); }
}
struct QKOff { unsigned o0, o1, ox; };
__device__ __forceinline__ QKOff qk_offsets(int T, int tile, int lane, unsigned base) {
    const int h = lane >> 5, r = lane & 31;
    QKOff o; o.ox = 0x80000000u;
#pragma unroll
    for (int p = 0; p < 2; ++p) {
        const int C = 4 * T + 2 * p + h, e = (C * 9363) >> 16, c = C - 7 * e, hd = 2 * e + (c > 3), u = c & 3;
        const unsigned off = base + hd * HS + tile * TS + u * 512 + r * 16;
        if (p == 0) o.o0 = off; else o.o1 = off;
        o.ox = c == 3 ? off + HS : o.ox;
    }
    return o;
}
__device__ __forceinline__ void store_k(const f32x16& a, const QKOff& o, rsrc_t rs) {
    constexpr unsigned padlo = 0x3F80u;
    u32x4 v[2];
#pragma unroll
    for (int p = 0; p < 2; ++p) {
        const unsigned a0 = cvtpk(a[8 * p], a[8 * p + 1]), a1 = cvtpk(a[8 * p + 2], a[8 * p + 3]);
        const unsigned b0 = cvtpk(a[8 * p + 4], a[8 * p + 5]), b1 = cvtpk(a[8 * p + 6], a[8 * p + 7]);
        auto r0 = __builtin_amdgcn_permlane32_swap(a0, b0, false, false), r1 = __builtin_amdgcn_permlane32_swap(a1, b1, false, false);
        v[p] = (u32x4){r0[0], r1[0], r0[1], r1[1]};
    }
    const bool x0 = o.ox == o.o0 + HS, x1 = o.ox == o.o1 + HS;
    const u32x4 vx = {x0 ? v[0][2] : v[1][2], x0 ? v[0][3] : v[1][3], padlo, 0u};
    v[0][2] = x0 ? padlo : v[0][2]; v[0][3] = x0 ? 0u : v[0][3];
    v[1][2] = x1 ? padlo : v[1][2]; v[1][3] = x1 ? 0u : v[1][3];
    __builtin_amdgcn_raw_buffer_store_b128(v[0], rs, o.o0, 0, 16);
    __builtin_amdgcn_raw_buffer_store_b128(v[1], rs, o.o1, 0, 16);
    __builtin_amdgcn_raw_buffer_store_b128(vx, rs, o.ox, 0, 16);
}
__device__ __forceinline__ unsigned v_offset(int T, int tile, int lane) {
    const int h = lane >> 5, col = 32 * T + (lane & 31), hd = (col * 2341) >> 16, d = col - 28 * hd;
    return WS_V + hd * HS + tile * TS + (h * 32 + d) * 16;
}
__device__ __forceinline__ void store_v(const f32x16& a, unsigned off, rsrc_t rs) {
#pragma unroll
    for (int s = 0; s < 2; ++s) {
        u32x4 v = {cvtpk(a[8 * s], a[8 * s + 1]), cvtpk(a[8 * s + 2], a[8 * s + 3]), cvtpk(a[8 * s + 4], a[8 * s + 5]), cvtpk(a[8 * s + 6], a[8 * s + 7])};
        __builtin_amdgcn_raw_buffer_store_b128(v, rs, off + s * 1024, 0, 16);
    }
}
__device__ __forceinline__ u32x4 pack8(const float (&hv)[8]) {
    return (u32x4){cvtpk(hv[0], hv[1]), cvtpk(hv[2], hv[3]), cvtpk(hv[4], hv[5]), cvtpk(hv[6], hv[7])};
}
__device__ __forceinline__ unsigned lds_arrive(lptr smem, int idx) {
    unsigned old;
    asm volatile("s_mov_b64 exec, 1\n\tds_add_rtn_u32 %0, %1, %2\n\ts_mov_b64 exec, -1\n\ts_waitcnt lgkmcnt(0)" : "=&v"(old) : "v"(smem + L_CTL + idx * 4), "v"(1u) : "memory");
    return __builtin_amdgcn_readfirstlane(old);
}
__device__ __forceinline__ void lds_post(lptr smem, int idx) {
    asm volatile("s_waitcnt lgkmcnt(0)\n\ts_mov_b64 exec, 1\n\tds_add_u32 %0, %1\n\ts_mov_b64 exec, -1" ::"v"(smem + L_CTL + idx * 4), "v"(1u) : "memory");
}
template <int SLEEP = 0>
__device__ __forceinline__ void lds_wait(lptr smem, int idx, unsigned want) {
#pragma unroll 1
    for (unsigned spins = 0; spins < (1u << 22); ++spins) {
        const unsigned v = __hip_atomic_load((LAS unsigned*)(smem + L_CTL) + idx, __ATOMIC_RELAXED, __HIP_MEMORY_SCOPE_WORKGROUP);
        if (__builtin_amdgcn_readfirstlane(v) >= want) break;
        if constexpr (SLEEP > 0) __builtin_amdgcn_s_sleep(SLEEP);
    }
    asm volatile("" ::: "memory");
}
constexpr unsigned POISON = 0xFFFFFFFFu;

__device__ __forceinline__ void load_epoch(u64& e, const char* ws) {
    asm volatile("global_load_dwordx2 %0, %1, %2 sc1" : "=v"(e) : "v"(WS_EPOCH), "s"(ws) : "memory");
}
struct AAddr { lptr xa[4]; lptr t0, t1, t2; };

constexpr int p1_wait(int st) { return 5 + (st >= 4) + (st == 5); }
template <bool V, int N, class F>
__device__ __forceinline__ void p1_steps(bf16x8 (&xf)[28], bf16x8 (&wf)[2][3], f32x16 (&acc)[3], const ASrc& s, const AAddr& a, AAddr& c, u64& tag, char* ws, F& offsets) {
    constexpr int st = N / 4, ks = N % 4, M = N - 1, sb = (st % 3) * 40960;
    const f32x16 z = {};
    if constexpr (N < 28 && ks == 0) {
        c.t0 = a.t0 + sb; c.t1 = a.t1 + sb; c.t2 = a.t2 + sb; PIN(c.t0); PIN(c.t1); PIN(c.t2);
        wait_bar<p1_wait(st)>();
    }
    if constexpr (N > 0) acc[0] = V ? MFMA(xf[M], wf[M & 1][0], M == 0 ? z : acc[0]) : MFMA(wf[M & 1][0], xf[M], M == 0 ? z : acc[0]);
    SBAR();
    if constexpr (N < 28) {
        xf[N] = *(LAS bf16x8*)(a.xa[ks] + sb);
        wf[N & 1][0] = trfrag(c.t0 + ks * 2048, c.t0 + ks * 2048 + 512);
    }
    SBAR();
    if constexpr (N > 0) acc[1] = V ? MFMA(xf[M], wf[M & 1][1], M == 0 ? z : acc[1]) : MFMA(wf[M & 1][1], xf[M], M == 0 ? z : acc[1]);
    SBAR();
    if constexpr (N < 28) {
        wf[N & 1][1] = trfrag(c.t1 + ks * 2048, c.t1 + ks * 2048 + 512);
        wf[N & 1][2] = trfrag(c.t2 + ks * 2048, c.t2 + ks * 2048 + 512);
    }
    SBAR();
    if constexpr (N > 0) acc[2] = MFMA(wf[M & 1][2], xf[M], M == 0 ? z : acc[2]);
    SBAR();
    if constexpr (N < 28) {
        constexpr int u0 = 10 + 5 * st;
        if constexpr (N == 16) load_epoch(tag, ws);
        if constexpr (N == 24) PIN(tag);
        if constexpr (st == 6) {
            if constexpr (ks == 0) { if (!V && s.hf) dma16(s.vq, s.wq + 20 * (16 * 2688), s.ldsw + L_WQ + 20 * 1024); issue_unit<u0>(s); }
        } else {
            if constexpr (ks == 0) issue_units<u0, u0 + 2>(s);
            else issue_unit<u0 + 1 + ks>(s);
            if constexpr (st >= 3 && ks == 2) dma16(s.vq, s.wq + (st - 3) * (128 * 2688), s.ldsw + L_WQ + (st - 3) * 8192);
        }
        if constexpr (N == 6 || N == 10) offsets(V, N == 10);
    }
    SBAR();
    if constexpr (N < 28) p1_steps<V, N + 1>(xf, wf, acc, s, a, c, tag, ws, offsets);
}

struct DSrc { const char* wd; const char* wo; unsigned vd, ring; };
__device__ __forceinline__ void ring_fill(const DSrc& d, const char* src, int slot) {
    dma16(d.vd, src, d.ring + slot * 2048);
    dma16(d.vd, src + 8 * 896, d.ring + slot * 2048 + 1024);
}

__attribute__((always_inline)) constexpr int p2_k(int i) { return i < 48 ? i / 4 : 12 + ((i - 48) % 32) / 2; }
__attribute__((always_inline)) constexpr int p2_b(int i) { return i < 48 ? i % 4 : 2 * ((i - 48) / 32) + (i & 1); }
struct Silu { float e, r, prev; };
template <int N, class F>
__device__ __forceinline__ void p2_steps(const bf16x8 (&xf)[28], bf16x8 (&wf)[7], f32x16 (&ac2)[4], lptr tb, int e1, int e2, lptr (&c)[4], Silu& p, u32x4 (&hq)[2], F& hook) {
    constexpr int R = N + 6, rk = p2_k(R), rb = p2_b(R), st = rk / 4, ks = rk % 4;
    const f32x16 z = {};
    hook(std::integral_constant<int, N>{});
    if constexpr (R < 112 && ks == 0) { c[rb] = tb + (upos(35 + 2 * st) + ((rb & 1) ? e1 : 0) + ((rb & 2) ? e2 : 0)); PIN(c[rb]); }
    if constexpr (N >= 0 && N < 112) ac2[p2_b(N)] = MFMA(wf[N % 7], xf[p2_k(N)], N < 4 ? z : ac2[p2_b(N)]);
    SBAR();
    if constexpr (R < 112) wf[R % 7] = trfrag(c[rb] + ks * 2048, c[rb] + ks * 2048 + 512);
    constexpr int E = (N - 82) / 2;
    if constexpr (N >= 82 && (N & 1) == 0) {
        if constexpr (E >= 1) {
            constexpr int j = (E - 1) % 8, t = (E - 1) / 8;
            const float hcur = ac2[t][j] * p.r * ac2[t][8 + j];
            if constexpr (j & 1) { hq[t][j >> 1] = cvtpk(p.prev, hcur); PIN(hq[t][j >> 1]); } else { p.prev = hcur; PIN(p.prev); }
        }
        if constexpr (E < 16) { p.e = EXP2(ac2[E / 8][E % 8] * -1.4426950408889634f); PIN(p.e); }
    }
    if constexpr (N >= 83 && (N & 1) == 1 && E < 16) { p.r = __builtin_amdgcn_rcpf(1.0f + p.e); PIN(p.r); }
    SBAR();
    if constexpr (N < 114) p2_steps<N + 1>(xf, wf, ac2, tb, e1, e2, c, p, hq, hook);
}
template <int N, class F>
__device__ __forceinline__ void q_steps(const bf16x8 (&xf)[28], bf16x8 (&wf)[6], f32x16& a2, lptr tq, F& publish) {
    constexpr int R = N + 5;
    const f32x16 z = {};
    if constexpr (N >= 0) a2 = MFMA(wf[N % 6], xf[N], N == 0 ? z : a2);
    SBAR();
    if constexpr (R < 28) wf[R % 6] = trfrag(tq + R * 1024, tq + R * 1024 + 256);
    if constexpr (N == 14) { PIN(a2); publish(); }
    SBAR();
    if constexpr (N < 27) q_steps<N + 1>(xf, wf, a2, tq, publish);
}
__device__ __forceinline__ u32x4 silu_tile(const f32x16& t) {
    float hv[8];
#pragma unroll
    for (int j = 0; j < 8; ++j) hv[j] = silu_mul(t[j], t[8 + j]);
    return pack8(hv);
}

constexpr int grp_sz(int m) { return (m >= 14 && m < 28) ? 4 : 2; }
constexpr int down_wait(int n) {
    int s = 0; for (int m = n + 2; m <= n + 6; ++m) s += grp_sz(m);
    return s;
}
__device__ __forceinline__ void hload(bf16x8& dst, unsigned voff, const char* base) {
    asm volatile("global_load_dwordx4 %0, %1, %2 sc1" : "=v"(dst) : "v"(voff), "s"(base) : "memory");
}
template <int N, class F>
__device__ __forceinline__ void down_steps(bf16x8 (&hf)[28][2], bf16x8 (&wf)[28][2], f32x16 (&acc)[2][2], const DSrc& d, lptr r0, lptr r1,
                                           const char* hb, unsigned hv0, unsigned hv1, F& hook) {
    if constexpr (N + 1 < 28) {
        asm volatile("s_waitcnt vmcnt(%2)" : "+v"(hf[N + 1][0]), "+v"(hf[N + 1][1]) : "i"(down_wait(N)) : "memory");
        if constexpr (N + 1 >= 7) {
            constexpr int so = ((N + 1) % 7) * 2048;
            wf[N + 1][0] = trfrag(r0 + so, r0 + so + 1024);
            wf[N + 1][1] = trfrag(r1 + so, r1 + so + 1024);
        }
    }
    SBAR();
    const f32x16 z = {};
    acc[0][0] = MFMA(wf[N][0], hf[N][0], N == 0 ? z : acc[0][0]);
    acc[0][1] = MFMA(wf[N][1], hf[N][0], N == 0 ? z : acc[0][1]);
    acc[1][0] = MFMA(wf[N][0], hf[N][1], N == 0 ? z : acc[1][0]);
    acc[1][1] = MFMA(wf[N][1], hf[N][1], N == 0 ? z : acc[1][1]);
    SBAR();
    if constexpr (N == 1) hook();
    constexpr int M = N + 7;
    if constexpr (M >= 14) ring_fill(d, M < 28 ? d.wd + M * 16 * 896 : d.wo + (M - 28) * 16 * 896, M % 7);
    if constexpr (M < 28) { hload(hf[M][0], hv0, hb + M * 1024); hload(hf[M][1], hv1, hb + M * 1024); }
    if constexpr (N + 1 < 28) down_steps<N + 1>(hf, wf, acc, d, r0, r1, hb, hv0, hv1, hook);
}

__device__ __forceinline__ bool poisoned(const u32x4& u, const u32x4& v) {
    unsigned mx = __builtin_elementwise_max(__builtin_elementwise_max(u[0], u[1]), __builtin_elementwise_max(u[2], u[3]));
    mx = __builtin_elementwise_max(mx, __builtin_elementwise_max(__builtin_elementwise_max(v[0], v[1]), __builtin_elementwise_max(v[2], v[3])));
    return ANY(mx == POISON);
}
template <int I>
__device__ __forceinline__ void o_steps(u32x4 (&of)[16], const bf16x8 (&wf)[8][4], f32x16& a0, f32x16& a1, rsrc_t rs, unsigned lv, unsigned so) {
    asm volatile("s_waitcnt vmcnt(%2)" : "+v"(of[2 * I]), "+v"(of[2 * I + 1]) : "i"(14 - 2 * I) : "memory");
    if (__builtin_expect(poisoned(of[2 * I], of[2 * I + 1]), 0)) {
        unsigned spins = 0;
        do {
            asm volatile("buffer_load_dwordx4 %0, %2, %3, %4 offen sc1\n\tbuffer_load_dwordx4 %1, %2, %3, %4 offen offset:1024 sc1\n\ts_waitcnt vmcnt(0)"
                         : "=&v"(of[2 * I]), "=&v"(of[2 * I + 1]) : "v"(lv), "s"(rs), "s"(so + I * 2048) : "memory");
        } while (poisoned(of[2 * I], of[2 * I + 1]) & (++spins < (1u << 18)));
    }
    a1 = MFMA(wf[I][1], __builtin_bit_cast(bf16x8, of[2 * I]), a1); a0 = MFMA(wf[I][0], __builtin_bit_cast(bf16x8, of[2 * I]), a0);
    a1 = MFMA(wf[I][3], __builtin_bit_cast(bf16x8, of[2 * I + 1]), a1); a0 = MFMA(wf[I][2], __builtin_bit_cast(bf16x8, of[2 * I + 1]), a0);
    SBAR();
    if constexpr (I < 7) o_steps<I + 1>(of, wf, a0, a1, rs, lv, so);
}

#define BF(x) __builtin_bit_cast(bf16x8, x)
#define E2(T, j, S, i) do { T[j] = EXP2(S[i]); T[j + 1] = EXP2(S[i + 1]); PIN(T[j]); PIN(T[j + 1]); SBAR(); } while (0)
#define C2(P, k, T) do { P[k] = cvtpk(T[0], T[1]); P[k + 1] = cvtpk(T[2], T[3]); PIN(P); SBAR(); } while (0)
#define ASTEPX(SC, SN, KN, TN, VC, TC, VP, TP, X) do { \
    SN = MFMA(KN[0], q[TN][0], z16); SBAR(); X; SBAR(); E2(ta, 0, SC, 0); C2(phi, 2, tb); \
    SN = MFMA(KN[1], q[TN][1], SN); SBAR(); E2(ta, 2, SC, 2); E2(tb, 0, SC, 4); E2(tb, 2, SC, 6); C2(plo, 0, ta); E2(ta, 0, SC, 8); E2(ta, 2, SC, 10); \
    o[TP] = MFMA(VP[1], BF(phi), o[TP]); SBAR(); C2(plo, 2, tb); E2(tb, 0, SC, 12); \
    o[TC] = MFMA(VC[0], BF(plo), o[TC]); SBAR(); E2(tb, 2, SC, 14); C2(phi, 0, ta); } while (0)
#define ASTEP(SC, SN, KN, TN, VC, TC, VP, TP) ASTEPX(SC, SN, KN, TN, VC, TC, VP, TP, )
#define HAVE(K, V) do { asm volatile("" : "+v"(K[0]), "+v"(K[1]), "+v"(V[0]), "+v"(V[1])); SBAR(); } while (0)

struct Args { const void *x, *wqkv, *wo, *wg, *wu, *wd, *gw; void *out, *ws; };
#if defined(__HIP_DEVICE_COMPILE__)
__attribute__((target("no-packed-fp32-ops")))
#endif
__global__ __launch_bounds__(512, 2) void fused_block(Args ka) {
    const u16* x = (const u16*)ka.x; const u16* wqkv = (const u16*)ka.wqkv; const u16* wo = (const u16*)ka.wo; const u16* wg = (const u16*)ka.wg;
    const u16* wu = (const u16*)ka.wu; const u16* wd = (const u16*)ka.wd; const u16* gw = (const u16*)ka.gw; u16* out = (u16*)ka.out; char* ws = (char*)ka.ws;
    __shared__ __attribute__((aligned(1024))) char smem_[163840];
    const lptr smem = (lptr)smem_;
    const int tid = threadIdx.x, lane = tid & 63, w = __builtin_amdgcn_readfirstlane(tid >> 6);
    const int h = lane >> 5, r = lane & 31, q4 = (lane & 15) >> 2, p4 = lane & 3, cg = (lane >> 4) & 1;
    unsigned pfv = 0;
    if (blockIdx.x < 32) {
        u64 pc; asm volatile("s_getpc_b64 %0" : "=s"(pc));
        asm volatile("global_load_dword %0, %1, %2" : "+v"(pfv) : "v"(imin((int)((blockIdx.x >> 3) * 512 + tid), CODE_LINES - 1) * 128), "s"(pc) : "memory");
    }
    const int xcd = blockIdx.x & 7, slot = ((blockIdx.x >> 3) + 28) & 31;
    const int ai = a_role(xcd, slot) & 15, ac = a_role(xcd, slot) >> 4;
    const int rt = w & 3, hf = w >> 2;
    const bool gu = ac < 14;
    ASrc s;
    {
        const int rowl = 8 * w + (lane >> 3), c16 = lane & 7, sw = c16 ^ (cg << 2);
        s.vx = rowl * 896 + ((c16 ^ (((w & 1) << 2) | (lane >> 4))) << 4);
        s.vw0 = rowl * 2688 + sw * 16;
        s.vw1 = rowl * 3584 + sw * 16;
        s.vw2 = rowl * 3584 + (sw ^ 2) * 16;
        s.ldsw = (unsigned)(uintptr_t)smem + w * 1024;
        unsigned o0 = ac * 128 + 896, o1 = ac * 256, xo = ai * (128 * 896);
        PIN(s.vx); PIN(s.vw0); PIN(s.vw1); PIN(s.vw2);
        asm volatile("" : "+s"(o0), "+s"(o1), "+s"(xo), "+s"(s.ldsw));
        asm volatile("" ::"s"(x), "s"(wqkv), "s"(wo), "s"(wg), "s"(wu), "s"(wd), "s"(gw), "s"(out), "s"(ws));
        s.xb = (const char*)x + xo;
        s.wb[0] = (const char*)wqkv + o0;
        s.wb[1] = (const char*)wg + o1;
        s.wb[2] = (const char*)wu + o1;
        if (gu) {
            const char* pfb = rt == 0 ? (const char*)wqkv : (rt & 1) ? (const char*)wg : (const char*)wu;
            const int n = rt == 0 ? 21 : 28;
            asm volatile("global_load_dword %0, %1, %2" : "+v"(pfv) : "v"((((ac * 16 + ai) * 2 + hf) * n + (rt == 3 ? 0 : rt == 0 ? 7 + imin(lane, 13) : 2 * imin(lane, 13))) * 128), "s"(pfb) : "memory");
        }
        if (gu) issue_units<0, 5>(s);
    }
    ((LAS unsigned*)(smem + L_CTL))[tid & 255] = 0u;
    __builtin_amdgcn_s_barrier();
    if (gu) issue_units<5, 10>(s);
    const int bj = (int)(0xDBFEC9A687543120ull >> (4 * ac)) & 15, bi = ai;
    const rsrc_t rs = __builtin_amdgcn_make_buffer_rsrc(ws, 0, WS_END, 0x00020000);
    u64 tag;
    const int crt = 4 * xcd + (slot * 37 >> 8), cct = slot - 7 * (slot * 37 >> 8);
    DSrc d;
    d.vd = (lane >> 3) * 896 + (((lane & 7) ^ (cg << 2)) << 4);
    d.wd = (const char*)wd + (rt * 448) * 896 + cct * 128;
    d.wo = (const char*)wo + (rt * 112) * 896 + cct * 128;
    d.ring = (unsigned)(uintptr_t)smem + RING + rt * 14336;
    bf16x8 xf[28];
    s.wq = (const char*)wqkv + (112 * (bj >> 1) + 48 * (bj & 1));
    s.vq = (16 * w + (lane >> 2)) * 2688 + (lane & 3) * 16;
    s.hf = hf;
#define FIX_TAG() do { tag = (((u64)(unsigned)__builtin_amdgcn_readfirstlane((unsigned)(tag >> 32)) << 32) | (unsigned)__builtin_amdgcn_readfirstlane((unsigned)tag)) + 0x9E3779B97F4A7C15ull; } while (0)
#define KILL(v) asm volatile("" : "=v"(v))
#define KILL_X() do { _Pragma("unroll") for (int n_ = 0; n_ < 28; ++n_) KILL(xf[n_]); } while (0)
#define ROLE(k) (({ asm volatile("" : "+s"(role)); role; }) == (k))
    {
        AAddr a, cur;
        const int f = (r >> 1) & 7;
#pragma unroll
        for (int k = 0; k < 4; ++k) a.xa[k] = smem + (32 * rt + r) * 128 + (((2 * k + h) ^ f) << 4);
        const int rowpart = (8 * h + q4) * 128 + p4 * 8, sx = q4 & 2;
        const int gub = rowpart + cg * 8192, gux = sx ^ cg;
        a.t0 = smem + (hf == 0 ? rowpart + ((cg ^ sx) << 5) + 16384 : gub + ((1 ^ gux) << 5) + 24576);
        a.t1 = smem + (hf == 0 ? rowpart + (((2 + cg) ^ sx) << 5) + 16384 : gub + ((2 ^ gux) << 5) + 24576);
        a.t2 = smem + gub + (((hf == 0 ? 0 : 3) ^ gux) << 5) + 24576;
        bf16x8 wf[2][3]; f32x16 acc[3];
        const int tile = 4 * ai + rt;
        QKOff ko0, ko1;
        auto offsets = [&](bool isv, bool second) __attribute__((always_inline)) {
            int ln = lane; PIN(ln);
            QKOff& k = second ? ko1 : ko0;
            if (isv) { k.o0 = v_offset(2 * (ac - 7) + second, tile, ln); PIN(k.o0); }
            else { k = qk_offsets(2 * ac + second, tile, ln, WS_K); PIN(k.o0); PIN(k.o1); PIN(k.ox); }
        };
        if (!gu) {
            unsigned pf2;
            {
                const int r0 = 14 * (ai * 2 + (ac - 14)), i = rt * 49 + lane, j = imin(w * 13 + lane - 49, 97), ir = (i * 4682) >> 16, jr = (j * 9363) >> 16;
                const char* pa = lane < 49 ? (const char*)(hf ? wu : wg) + (r0 + ir) * 3584 + (i - 14 * ir) * 256 + 128 : (const char*)wqkv + (r0 + jr) * 2688 + (j - 7 * jr) * 128;
                asm volatile("global_load_dword %0, %1, off" : "=v"(pf2) : "v"(pa) : "memory");
            }
#pragma unroll
            for (int u = 0; u < 14; ++u) { const int v = (u + 4) % 14; dma16(s.vx, s.xb + ((v & 1) * 64 * 896 + (v >> 1) * 128), s.ldsw + (1 + v) * 8192); }
            {
                const unsigned pw = (lane & 3) == 0 ? 0x3F803F80u : 0u;
                __builtin_amdgcn_raw_buffer_store_b128((u32x4){pw, pw, pw, pw}, rs, WS_V + (8 * (ac - 14) + w) * HS + (4 * ai + (lane >> 4)) * TS + ((lane >> 2) & 3) * 512 + (28 + (lane & 3)) * 16, 0, 16);
            }
#pragma unroll
            for (int k = 0; k < 3; ++k) dma16(s.vq, s.wq + k * (128 * 2688), s.ldsw + L_WQ + k * 8192);
            if (hf) dma16(s.vq, s.wq + 20 * (16 * 2688), s.ldsw + L_WQ + 20 * 1024);
            dma16((lane < 56 ? lane : 55) * 16, gw, (unsigned)(uintptr_t)smem + L_CTL + 1024);
            load_epoch(tag, ws);
            wait_bar<0>();
            PIN(tag); PIN(pf2);
#pragma unroll
            for (int n = 0; n < 28; ++n) xf[n] = *(LAS bf16x8*)(a.xa[n & 3] + (1 + 2 * (n >> 2)) * 8192);
        } else if (ac >= 7 && hf == 0) p1_steps<true, 0>(xf, wf, acc, s, a, cur, tag, ws, offsets);
        else p1_steps<false, 0>(xf, wf, acc, s, a, cur, tag, ws, offsets);
        asm volatile("" ::"v"(pfv));
        FIX_TAG();
        const unsigned hoff = WS_H + (tile * 112 + 8 * ac) * 1024 + lane * 16;
        int role = gu ? hf : 2;
        if (ROLE(0)) {
            if (ac < 7) { store_k(acc[0], ko0, rs); store_k(acc[1], ko1, rs); }
            else { store_v(acc[0], ko0.o0, rs); store_v(acc[1], ko1.o0, rs); }
            SBAR();
            *(LAS u32x4*)(smem + L_HS + rt * 1024 + lane * 16) = silu_tile(acc[2]);
            if (ac < 7) wait_bar<6>(); else wait_bar<4>();
            KILL(acc[0]); KILL(acc[1]); KILL(acc[2]);
        }
        if (ROLE(2)) {
            if (hf == 1) {
                unsigned pf[2];
                {
                    const int sid = (ai * 2 + (ac - 14)) * 4 + rt;
                    asm volatile("global_load_dword %0, %1, %2" : "=v"(pf[0]) : "v"((sid * 98 + lane) * 128), "s"(wd) : "memory");
                    const char* pa = lane < 34 ? (const char*)wd + (sid * 98 + 64 + lane) * 128 : (const char*)wo + imin(sid * 25 + imin(lane - 34, 24), 3135) * 128;
                    asm volatile("global_load_dword %0, %1, off" : "=v"(pf[1]) : "v"(pa) : "memory");
                }
#pragma unroll
                for (int k = 0; k < 16; ++k)
                    __builtin_amdgcn_raw_buffer_store_b128((u32x4){POISON, POISON, POISON, POISON}, rs, lane * 16 + (k & 3) * 1024, WS_O + ai * 131072 + (ac - 14) * 65536 + rt * 16384 + (k >> 2) * 4096, 16);
                asm volatile("s_waitcnt vmcnt(16)" : "+v"(pf[0]), "+v"(pf[1])::"memory");
                if (ac == 15) {
                    typedef __attribute__((ext_vector_type(2))) __bf16 b2;
                    float sum = 0.f;
#pragma unroll
                    for (int n = 0; n < 28; ++n) {
                        const u32x4 gv = *(LAS u32x4*)(smem + L_CTL + 1024 + 32 * n + 16 * h); const u32x4 xv = __builtin_bit_cast(u32x4, xf[n]);
#pragma unroll
                        for (int k = 0; k < 4; ++k) { const unsigned xs = xv[k], gs = gv[k]; sum = __builtin_amdgcn_fdot2_f32_bf16(__builtin_bit_cast(b2, xs), __builtin_bit_cast(b2, gs), sum, false); }
                        if (n % 4 == 3) SBAR();
                    }
                    auto rr = __builtin_amdgcn_permlane32_swap(F2U(sum), F2U(sum), false, false);
                    sum = U2F(rr[0]) + U2F(rr[1]);
                    const float gv = __builtin_amdgcn_rcpf(1.0f + EXP2(sum * -1.4426950408889634f));
                    __builtin_amdgcn_raw_buffer_store_b32(F2U(gv), rs, h ? 0x80000000u : WS_G + (tile * 32 + r) * 4, 0, 16);
                }
            }
        }
        if (ROLE(1)) {
            wait_bar<0>();
            u32x4 hp[4];
#pragma unroll
            for (int t = 0; t < 4; ++t) {
#pragma unroll
                for (int i = 4 * t; i < 4 * t + 4; ++i) {
                    const int m = 6 + i / 2, half = i & 1;
                    dma16(m % 2 == 0 ? s.vw1 : s.vw2, s.wb[1 + m % 2] + (128 + (m / 2) * (64 * 3584) - half * (32 * 3584)), s.ldsw + upos(35 + m) - half * 4096);
                }
                if (t < 3) { hp[t + 1] = silu_tile(acc[t]); PIN(hp[t + 1]); }
            }
            hp[0] = *(LAS u32x4*)(smem + L_HS + rt * 1024 + lane * 16);
            f32x16 ac2[4]; lptr c2[4]; bf16x8 w2[7]; Silu sp; u32x4 hq[2];
            auto hook = [&](auto n) __attribute__((always_inline)) {
                if constexpr (n.value >= 8 && n.value < 24 && n.value % 4 == 0) __builtin_amdgcn_raw_buffer_store_b128(hp[n.value / 4 - 2], rs, hoff + (n.value / 4 - 2) * 1024, 0, 16);
                if constexpr (n.value == 34) { asm volatile("s_waitcnt vmcnt(4)" ::: "memory"); lds_post(smem, C_ST); }
                if constexpr (n.value == 42) lds_wait(smem, C_ST, 4);
                if constexpr (n.value == 99) __builtin_amdgcn_raw_buffer_store_b128(hq[0], rs, hoff + 4 * 1024, 0, 16);
                if constexpr (n.value == 80) __builtin_amdgcn_s_setprio(1);
            };
            p2_steps<-6>(xf, w2, ac2, smem + gub + (gux << 5), cg ? -32 : 32, sx ? -64 : 64, c2, sp, hq, hook);
            __builtin_amdgcn_raw_buffer_store_b128(hq[1], rs, hoff + 5 * 1024, 0, 16);
            __builtin_amdgcn_s_setprio(1);
            lds_post(smem, C_P2);
            lds_wait(smem, C_P2, 4);
            __builtin_amdgcn_s_setprio(0);
#pragma unroll
            for (int k = 0; k < 7; ++k) ring_fill(d, d.wd + k * 16 * 896, k);
            __builtin_amdgcn_s_setprio(1);
#pragma unroll
            for (int t = 2; t < 4; ++t) __builtin_amdgcn_raw_buffer_store_b128(silu_tile(ac2[t]), rs, hoff + (4 + t) * 1024, 0, 16);
            KILL_X();
        }
    }
    bf16x8 q00, q01, k0[2], k1[2], k2[2], v0[2], v1[2], v2[2];
    const unsigned lv = lane * 16, kb = WS_K + bj * HS;
    unsigned ko = kb + rt * (16 * TS);
#define LDT(K, V, so) do { const unsigned so_ = (so); K[0] = ld_frag(rs, lv, so_); K[1] = ld_frag(rs, lv + 1024, so_); V[0] = ld_frag(rs, lv + 2048, so_); V[1] = ld_frag(rs, lv + 3072, so_); } while (0)
    int role = hf;
    if (ROLE(0)) {
        __builtin_amdgcn_s_setprio(2);
        const int u = 2 * cg + (p4 & 1), jj = p4 >> 1, wqd = (2 * u + jj + 2 * (bj & 1)) ^ ((bj & 1) && u == 3 ? 9 : 0);
        lptr tq = smem + L_WQ + (8 * h + q4) * 64 + wqd * 8;
        const int uk = (28 * bj + ((lane & 1) ? 27 : 0)) >> 6;
        unsigned fo = WS_F1 + (lane < 16 ? (uk + ((lane & 2) ? 7 : 0)) * 16 + 4 * w + (lane >> 2) : (14 + (bj >> 3)) * 16 + 4 * w + (lane & 3)) * FS;
        PIN(tq); PIN(fo);
        f32x16 a2; bf16x8 w6[6];
        auto publish = [&]() __attribute__((always_inline)) {
            asm volatile("s_waitcnt vmcnt(0)" ::: "memory");
            if (lds_arrive(smem, C_KV) == 3u) set_flag(ws, WS_F1 + (ac * 16 + ai) * FS, tag);
        };
        q_steps<-5>(xf, w6, a2, tq, publish);
        PIN(a2);
        const float qs = 0.27264374f;
        u32x4 qa = {cvtpk(a2[0] * qs, a2[1] * qs), cvtpk(a2[2] * qs, a2[3] * qs), cvtpk(a2[4] * qs, a2[5] * qs), cvtpk(a2[6] * qs, a2[7] * qs)};
        u32x4 qb = {cvtpk(a2[8] * qs, a2[9] * qs), cvtpk(a2[10] * qs, a2[11] * qs), cvtpk(a2[12] * qs, a2[13] * qs), cvtpk(a2[14] * qs, a2[15] * qs)};
        qb[2] = h ? 0u : qb[2]; qb[3] = h ? 0u : qb[3];
        q00 = BF(qa); q01 = BF(qb);
        *(LAS bf16x8*)(smem + L_QX + w * 2048 + lv) = q00; *(LAS bf16x8*)(smem + L_QX + w * 2048 + 1024 + lv) = q01;
        lds_post(smem, C_QX);
        f32x16 o[4] = {}; u32x4 phi = {0u, 0u, 0u, 0u}; float tb[4] = {0.f, 0.f, 0.f, 0.f};
        PIN(o[0]); PIN(o[1]); PIN(o[2]); PIN(o[3]); PIN(phi); PIN(tb[0]); PIN(tb[1]); PIN(tb[2]); PIN(tb[3]);
        bf16x8 q[4][2], k3[2], v3[2];
        const f32x16 z16 = {};
        q[0][0] = q00; q[0][1] = q01;
        __builtin_amdgcn_s_setprio(0);
        lds_wait<1>(smem, C_QX, 4);
#pragma unroll
        for (int t = 1; t < 4; ++t) { q[t][0] = *(LAS bf16x8*)(smem + L_QX + ((t + w) & 3) * 2048 + lv); q[t][1] = *(LAS bf16x8*)(smem + L_QX + ((t + w) & 3) * 2048 + 1024 + lv); }
        lds_post(smem, C_QRD);
        poll_flags(ws, fo & ~(3u * FS), tag);
        __builtin_amdgcn_s_setprio(2);
        LDT(k0, v0, ko);
        const u64 fv = __hip_atomic_load((GAS u64*)(ws + fo), RLX_AGENT);
        LDT(k1, v1, ko + TS); LDT(k2, v2, ko + 2 * TS);
        {
            f32x16 sr = MFMA(k0[0], q[0][0], z16); sr = MFMA(k0[1], q[0][1], sr);
            const unsigned mb = cvtpk(-swap_max(max16(sr)), 0.f) & 0xFFFFu;
            *(LAS unsigned*)(smem + L_M + w * 128 + r * 4) = mb | 0x10000u;
            u32x4 qq = __builtin_bit_cast(u32x4, q[0][1]); qq[2] = h ? mb : qq[2]; q[0][1] = BF(qq);
        }
        f32x16 sa, sb; u32x4 plo; float ta[4];
        v3[0] = k0[0]; v3[1] = k0[1];
        sa = MFMA(k0[0], q[0][0], z16); sa = MFMA(k0[1], q[0][1], sa);
        SBAR();
        {
            unsigned mb[4];
#pragma unroll 1
            for (unsigned spins = 0; spins < (1u << 22); ++spins) {
#pragma unroll
                for (int t = 1; t < 4; ++t) mb[t] = *(volatile LAS unsigned*)(smem + L_M + ((t + w) & 3) * 128 + r * 4);
                if (ALL((mb[1] & mb[2] & mb[3] & 0x10000u) != 0u)) break;
            }
#pragma unroll
            for (int t = 1; t < 4; ++t) { u32x4 qq = __builtin_bit_cast(u32x4, q[t][1]); qq[2] = h ? mb[t] & 0xFFFFu : qq[2]; q[t][1] = BF(qq); }
        }
        __builtin_amdgcn_s_setprio(0);
        if (!ALL(fv == tag)) poll_flags(ws, fo, tag);
        __builtin_amdgcn_fence(__ATOMIC_ACQUIRE, "wavefront");
#define PUT(t) do { lptr p_ = smem + L_ATT + (((w + (t) + 1) & 3) * 3 + (t)) * 4096 + lv; \
            _Pragma("unroll") for (int g_ = 0; g_ < 4; ++g_) *(LAS f32x4*)(p_ + g_ * 1024) = (f32x4){o[t][4 * g_], o[t][4 * g_ + 1], o[t][4 * g_ + 2], o[t][4 * g_ + 3]}; } while (0)
        for (int it = 0; it < 4; ++it) {
            SBAR();
            ASTEP(sa, sb, k0, 1, v0, 0, v3, 3);
            ASTEPX(sb, sa, k0, 2, v0, 1, v0, 0, LDT(k3, v3, ko + 3 * TS); ko += it == 3 ? 2 * WS_END : 4 * TS);
            ASTEP(sa, sb, k0, 3, v0, 2, v0, 1); HAVE(k1, v1); ASTEP(sb, sa, k1, 0, v0, 3, v0, 2);
            ASTEP(sa, sb, k1, 1, v1, 0, v0, 3);
            ASTEPX(sb, sa, k1, 2, v1, 1, v1, 0, LDT(k0, v0, ko)); ASTEP(sa, sb, k1, 3, v1, 2, v1, 1); HAVE(k2, v2); ASTEP(sb, sa, k2, 0, v1, 3, v1, 2);
            ASTEP(sa, sb, k2, 1, v2, 0, v1, 3);
            ASTEPX(sb, sa, k2, 2, v2, 1, v2, 0, LDT(k1, v1, ko + TS)); ASTEP(sa, sb, k2, 3, v2, 2, v2, 1); HAVE(k3, v3); ASTEP(sb, sa, k3, 0, v2, 3, v2, 2);
            ASTEP(sa, sb, k3, 1, v3, 0, v2, 3);
            ASTEPX(sb, sa, k3, 2, v3, 1, v3, 0, LDT(k2, v2, ko + 2 * TS)); ASTEP(sa, sb, k3, 3, v3, 2, v3, 1);
            if (it == 3) { __builtin_amdgcn_s_setprio(2); lds_wait(smem, C_G, 4); PUT(0); }
            HAVE(k0, v0); ASTEP(sb, sa, k0, 0, v3, 3, v3, 2);
        }
        phi[2] = cvtpk(tb[0], tb[1]); phi[3] = cvtpk(tb[2], tb[3]);
        o[3] = MFMA(v3[1], BF(phi), o[3]);
        PUT(1); PUT(2);
        lds_post(smem, C_ATT);
        const unsigned gb = *(LAS unsigned*)(smem + L_GV + ((w + 3) & 3) * 128 + r * 4);
        lds_wait(smem, C_ATT, 4);
        f32x16 ot = o[3];
#pragma unroll
        for (int t = 0; t < 3; ++t) {
            lptr p = smem + L_ATT + (w * 3 + t) * 4096 + lane * 16;
#pragma unroll
            for (int g = 0; g < 4; ++g) { const f32x4 v = *(LAS f32x4*)(p + g * 1024); ot[4 * g] += v[0]; ot[4 * g + 1] += v[1]; ot[4 * g + 2] += v[2]; ot[4 * g + 3] += v[3]; }
        }
        float l;
        { auto rr = __builtin_amdgcn_permlane32_swap(F2U(ot[12]), F2U(ot[12]), false, false); l = U2F(rr[1]); }
        float ck[4] = {l, 0.f, 0.f, 0.f};
#pragma unroll
        for (int i = 0; i < 16; ++i) ck[i & 3] = __builtin_fmaf(ot[i], 0.0f, ck[i & 3]);
        const float chk = (ck[0] + ck[1]) + (ck[2] + ck[3]);
        if (__builtin_expect(ANY(!(chk >= 0x1p-20f && chk <= 0x1p64f)), 0)) {
            u32x4 qq = __builtin_bit_cast(u32x4, q[3][1]); qq[2] = h ? 0u : qq[2];
            const bf16x8 qa = q[3][0], qb = BF(qq);
            float m = -INFINITY; ot = z16;
#pragma unroll 1
            for (int t = 0; t < 64; ++t) {
                const bf16x8 ka = ld_frag(rs, lv, kb + t * TS), kc = ld_frag(rs, lv + 1024, kb + t * TS);
                const bf16x8 va = ld_frag(rs, lv + 2048, kb + t * TS), vc = ld_frag(rs, lv + 3072, kb + t * TS);
                f32x16 sc = MFMA(ka, qa, z16); sc = MFMA(kc, qb, sc);
                const float mn = __builtin_fmaxf(m, swap_max(max16(sc))), al = EXP2(m - mn); m = mn;
#pragma unroll
                for (int i = 0; i < 16; ++i) { sc[i] = EXP2(sc[i] - mn); ot[i] *= al; }
                const u32x4 pa = {cvtpk(sc[0], sc[1]), cvtpk(sc[2], sc[3]), cvtpk(sc[4], sc[5]), cvtpk(sc[6], sc[7])};
                const u32x4 pb = {cvtpk(sc[8], sc[9]), cvtpk(sc[10], sc[11]), cvtpk(sc[12], sc[13]), cvtpk(sc[14], sc[15])};
                ot = MFMA(va, BF(pa), ot); ot = MFMA(vc, BF(pb), ot);
            }
            auto rr = __builtin_amdgcn_permlane32_swap(F2U(ot[12]), F2U(ot[12]), false, false); l = U2F(rr[1]);
        }
        const float rl = __builtin_amdgcn_rcpf(l) * U2F(gb);
        u32x4 o0 = {cvtpk(ot[0] * rl, ot[1] * rl), cvtpk(ot[2] * rl, ot[3] * rl), cvtpk(ot[4] * rl, ot[5] * rl), cvtpk(ot[6] * rl, ot[7] * rl)};
        u32x4 o1 = {cvtpk(ot[8] * rl, ot[9] * rl), cvtpk(ot[10] * rl, ot[11] * rl), cvtpk(ot[12] * rl, ot[13] * rl), cvtpk(ot[14] * rl, ot[15] * rl)};
        if (h) { o1[2] = 0u; o1[3] = 0u; }
#pragma unroll
        for (int k = 0; k < 4; ++k) { o0[k] = __builtin_elementwise_min(o0[k], POISON - 1u); o1[k] = __builtin_elementwise_min(o1[k], POISON - 1u); }
        const unsigned oo = WS_O + ((4 * bi + ((w + 3) & 3)) * 16 + bj) * 2048 + lv;
        __builtin_amdgcn_raw_buffer_store_b128(o0, rs, oo, 0, 16);
        __builtin_amdgcn_raw_buffer_store_b128(o1, rs, oo + 1024, 0, 16);
    }
    if (!ROLE(1)) return;
    asm volatile("s_waitcnt vmcnt(0)" ::: "memory");
    if (lds_arrive(smem, C_H) == 3) {
        set_flag(ws, WS_F2 + (ai * 16 + ac) * FS, tag);
    }
    if (!gu) {
        poll_flags(ws, WS_F2 + (bi * 16 + 14 + (lane & 1)) * FS, tag);
        *(LAS unsigned*)(smem + L_GV + rt * 128 + r * 4) = __builtin_amdgcn_raw_buffer_load_b32(rs, WS_G + (128 * bi + 32 * rt + r) * 4, 0, 16);
        lds_post(smem, C_G);
        if (blockIdx.x == 0 && w == 4) {
#pragma unroll 1
            for (int k = 0; k < 4; ++k) poll_flags<8>(ws, WS_F2 + (k * 64 + lane) * FS, tag);
            set_flag(ws, WS_EPOCH, tag);
        }
        return;
    }
    const int rq = rt & 1, cb0 = rt >> 1;
    f32x16 bq; float g; unsigned pw;
    {
        bf16x8 hfr[28][2], wf[28][2]; f32x16 acc[2][2];
        const lptr rb = smem + RING + rt * 14336 + (4 * h + q4) * 128 + cg * 32 + p4 * 8;
        const lptr r0 = rb + ((cb0 ^ (q4 >> 1)) << 6), r1 = rb + ((cb0 ^ 1 ^ (q4 >> 1)) << 6);
#pragma unroll
        for (int k = 0; k < 7; ++k) { wf[k][0] = trfrag(r0 + k * 2048, r0 + k * 2048 + 1024); wf[k][1] = trfrag(r1 + k * 2048, r1 + k * 2048 + 1024); }
        asm volatile("s_waitcnt lgkmcnt(0)" ::: "memory");
        __builtin_amdgcn_s_setprio(0);
#pragma unroll
        for (int k = 0; k < 7; ++k) ring_fill(d, d.wd + (7 + k) * 16 * 896, k);
        __builtin_amdgcn_s_setprio(1);
        poll_flags<2>(ws, WS_F2 + (lane < 16 ? (crt >> 1) * 16 + lane : bi * 16 + 14 + (lane & 1)) * FS, tag);
        unsigned gq;
        asm volatile("global_load_dword %0, %2, %4 sc1\n\tglobal_load_dword %1, %3, %4 sc1" : "=&v"(gq), "=&v"(g)
                     : "v"(WS_G + (128 * bi + 32 * rt + r) * 4), "v"(WS_G + (64 * crt + 32 * rq + r) * 4), "s"(ws) : "memory");
        auto hook = [&]() __attribute__((always_inline)) {
            PIN(gq); PIN(g);
            __builtin_amdgcn_s_setprio(1);
            *(LAS unsigned*)(smem + L_GV + rt * 128 + r * 4) = gq;
            lds_post(smem, C_G);
            __builtin_amdgcn_s_setprio(0);
        };
        const char* hb = ws + WS_H + (2 * crt * 112 + 28 * rt) * 1024;
        const unsigned hv0 = lane * 16 + rq * (112 * 1024), hv1 = lane * 16 + (rq ^ 1) * (112 * 1024);
        { unsigned a_ = hv0, b_ = hv1; PIN(a_); PIN(b_); }
        __builtin_amdgcn_s_setprio(0);
#pragma unroll
        for (int n = 0; n < 7; ++n) { hload(hfr[n][0], hv0, hb + n * 1024); hload(hfr[n][1], hv1, hb + n * 1024); }
        asm volatile("s_waitcnt vmcnt(%2)" : "+v"(hfr[0][0]), "+v"(hfr[0][1]) : "i"(12) : "memory");
        down_steps<0>(hfr, wf, acc, d, r0, r1, hb, hv0, hv1, hook);
        __builtin_amdgcn_s_setprio(2);
        lds_wait(smem, C_QRD, 4);
#pragma unroll
        for (int t = 1; t < 4; ++t) {
            lptr p = smem + bred_off((rt ^ ((t >> 1) | ((t & 1) << 1))) * 3 + t - 1) + lane * 16;
            const f32x16& v = acc[t >> 1][t & 1];
#pragma unroll
            for (int g = 0; g < 4; ++g) *(LAS f32x4*)(p + g * 1024) = (f32x4){v[4 * g], v[4 * g + 1], v[4 * g + 2], v[4 * g + 3]};
        }
        asm volatile("s_waitcnt vmcnt(0)" ::: "memory");
        lds_post(smem, C_BRED);
        lds_wait<1>(smem, C_BRED, 4);
        asm volatile("buffer_load_dword %0, %1, %2, 0 offen sc1" : "=v"(pw) : "v"(WS_O + ((2 * crt + rq) * 16 + 8 * cb0 + (lane & 7)) * 2048 + 2032), "s"(rs) : "memory");
        bq = acc[0][0];
#pragma unroll
        for (int t = 0; t < 3; ++t) {
            lptr p = smem + bred_off(rt * 3 + t) + lane * 16;
#pragma unroll
            for (int g = 0; g < 4; ++g) { const f32x4 v = *(LAS f32x4*)(p + g * 1024); bq[4 * g] += v[0]; bq[4 * g + 1] += v[1]; bq[4 * g + 2] += v[2]; bq[4 * g + 3] += v[3]; }
        }
    }
#pragma unroll
    for (int i = 0; i < 16; ++i) bq[i] *= 1.0f - g;
    PIN(bq);
    {
        const lptr rb = smem + RING + (cb0 * 224 + 4 * h + q4) * 128 + cg * 32 + p4 * 8;
        const lptr p0 = rb + ((cb0 ^ (q4 >> 1)) << 6), p1 = rb + ((cb0 ^ 1 ^ (q4 >> 1)) << 6), l0 = p0 - h * 512, l1 = p1 - h * 512;
        bf16x8 wf[8][4];
#pragma unroll
        for (int hh = 0; hh < 8; ++hh) {
            wf[hh][0] = trfrag(p0 + hh * 3584, p0 + hh * 3584 + 1024); wf[hh][1] = trfrag(p1 + hh * 3584, p1 + hh * 3584 + 1024);
            wf[hh][2] = trfrag(p0 + hh * 3584 + 2048, l0 + hh * 3584 + 3072); wf[hh][3] = trfrag(p1 + hh * 3584 + 2048, l1 + hh * 3584 + 3072);
        }
        const unsigned so = WS_O + ((2 * crt + rq) * 16 + 8 * cb0) * 2048;
        asm volatile("s_waitcnt vmcnt(0)" : "+v"(pw)::"memory");
        if (!ALL(pw != POISON)) {
            const unsigned po = so + (lane & 7) * 2048 + 2032;
#pragma unroll 1
            for (unsigned spins = 0; spins < (1u << 18); ++spins) {
                const unsigned v = __builtin_amdgcn_raw_buffer_load_b32(rs, po, 0, 16);
                if (__builtin_popcountll(BALLOT(v != POISON)) >= 56) break;
                __builtin_amdgcn_s_sleep(2);
                asm volatile("" ::: "memory");
            }
        }
        f32x16 a0 = bq, a1 = {};
        u32x4 of[16];
#pragma unroll
        for (int i = 0; i < 8; ++i)
            asm volatile("buffer_load_dwordx4 %0, %2, %3, %4 offen sc1\n\tbuffer_load_dwordx4 %1, %2, %3, %4 offen offset:1024 sc1"
                         : "=&v"(of[2 * i]), "=&v"(of[2 * i + 1]) : "v"(lv), "s"(rs), "s"(so + i * 2048) : "memory");
        o_steps<0>(of, wf, a0, a1, rs, lv, so);
        { lptr p = smem + bred_off(rt * 3) + lane * 16;
#pragma unroll
          for (int gq = 0; gq < 4; ++gq) *(LAS f32x4*)(p + gq * 1024) = (f32x4){a1[4 * gq], a1[4 * gq + 1], a1[4 * gq + 2], a1[4 * gq + 3]}; }
        lds_post(smem, C_FIN + rt);
        lds_wait(smem, C_FIN + (rt ^ 2), 1);
        { lptr p = smem + bred_off((rt ^ 2) * 3) + lane * 16;
#pragma unroll
          for (int gq = 0; gq < 4; ++gq) { const f32x4 v = *(LAS f32x4*)(p + gq * 1024);
#pragma unroll
              for (int e = 0; e < 4; ++e) bq[4 * gq + e] = a0[4 * gq + e] + v[e]; } }
        u16* op = out + (64 * crt + 32 * rq + r) * 448 + 64 * cct + 32 * cb0 + 8 * h;
#pragma unroll
        for (int p = 0; p < 2; ++p) {
            const unsigned x0 = cvtpk(bq[8 * p], bq[8 * p + 1]), x1 = cvtpk(bq[8 * p + 2], bq[8 * p + 3]);
            const unsigned y0 = cvtpk(bq[8 * p + 4], bq[8 * p + 5]), y1 = cvtpk(bq[8 * p + 6], bq[8 * p + 7]);
            auto r0 = __builtin_amdgcn_permlane32_swap(x0, y0, false, false), r1 = __builtin_amdgcn_permlane32_swap(x1, y1, false, false);
            *(GAS u32x4*)(op + 16 * p) = (u32x4){r0[0], r1[0], r0[1], r1[1]};
        }
    }
}

extern "C" void kernel_launch(void* const* d_in, const int* in_sizes, int n_in, void* d_out, int out_size, void* d_ws, size_t ws_size,
                              hipStream_t stream) {
    Args a; a.x = d_in[0]; a.wqkv = d_in[1]; a.wo = d_in[2]; a.wg = d_in[4]; a.wu = d_in[5]; a.wd = d_in[6]; a.gw = d_in[7]; a.out = d_out; a.ws = d_ws;
    fused_block<<<256, 512, 0, stream>>>(a);
}
```
